```python
import torch

def _softmax(x, axis=-1):
    return torch.softmax(x, dim=axis)
def _gelu(x):
    return torch.nn.functional.gelu(x, approximate="tanh")
def _silu(x):
    return torch.nn.functional.silu(x)
def _sigmoid(x):
    return torch.sigmoid(x)
def _layernorm(x, axis=-1, eps=1e-5):
    m = x.mean(dim=axis, keepdim=True)
    v = x.var(dim=axis, keepdim=True, unbiased=False)
    return (x - m) / torch.sqrt(v + eps)
def _scatter_add(tgt, idx, src, axis=0):
    return tgt.clone().index_add_(axis, idx, src)
def _segment_sum(src, seg, n):
    out = torch.zeros((n,) + src.shape[1:], dtype=src.dtype)
    return out.index_add_(0, seg, src)
def _pad(x, pw):
    flat = tuple(v for pair in reversed(pw) for v in pair)
    return torch.nn.functional.pad(x, flat)

kernel_name = 'parallel_sum__sliding_local_dilated__dense_activation_silu__prompt'

def setup_inputs():

    _w = (lambda a: (lambda t: t.to(torch.bfloat16) if t.is_floating_point() else t)(torch.as_tensor(a)))
    def _A_setup():
        B, S, D = 1, 2048, 512
        return {
            "x": _w((lambda *s: torch.randn(*s, dtype=torch.bfloat16) * 0.05)(B, S, D)),
            "wqkv": _w((lambda *s: torch.randn(*s, dtype=torch.bfloat16) * 0.05)(D, 3 * D)),
            "wo": _w((lambda *s: torch.randn(*s, dtype=torch.bfloat16) * 0.05)(D, D)),
        }
    _A_in = _A_setup()

    def _B_setup():
        B, S, D, F = 1, 2048, 512, 1792
        return {
            "x": _w((lambda *s: torch.randn(*s, dtype=torch.bfloat16) * 0.05)(B, S, D)),
            "w1": _w((lambda *s: torch.randn(*s, dtype=torch.bfloat16) * 0.8)(D, F) * (D ** -0.5)),
            "w2": _w((lambda *s: torch.randn(*s, dtype=torch.bfloat16) * 0.8)(F, D) * (F ** -0.5)),
        }
    _B_in = _B_setup()

    return {
        "A_x": _A_in["x"],
        "A_wqkv": _A_in["wqkv"],
        "A_wo": _A_in["wo"],
        "B_x": _B_in["x"],
        "B_w1": _B_in["w1"],
        "B_w2": _B_in["w2"],
    }

def reference(A_x, A_wqkv, A_wo, B_x, B_w1, B_w2):
    def _A_ref(x, wqkv, wo):
        B, S, D = x.shape
        H = 32; Dh = D // H
        W, dil = 8, 2
        qkv = (lambda x, *d: x.permute(*d))((x @ wqkv).reshape(B, S, 3, H, Dh), 2, 0, 3, 1, 4)
        q, k, v = qkv[0], qkv[1], qkv[2]
        scale = Dh ** -0.5
        pos = torch.arange(S)
        off = torch.arange(W) * dil
        idx = pos[:, None] - off[None, :]
        mask = idx >= 0
        idx_c = torch.where(mask, idx, 0).reshape(-1)
        kg = k[:, :, idx_c, :].reshape(B, H, S, W, Dh)
        vg = v[:, :, idx_c, :].reshape(B, H, S, W, Dh)
        logits = torch.einsum("bhsd,bhswd->bhsw", q, kg) * scale
        logits = torch.where(mask[None, None, :, :], logits, -1e9)
        attn = _softmax(logits, axis=-1)
        o = torch.einsum("bhsw,bhswd->bhsd", attn, vg)
        return (lambda x, *d: x.permute(*d))(o, 0, 2, 1, 3).reshape(B, S, D) @ wo

    def _B_ref(x, w1, w2):
        h = torch.einsum("bsd,df->bsf", x, w1)
        h = _silu(h)
        out = torch.einsum("bsf,fd->bsd", h, w2)
        return out

    x = A_x
    xn = _layernorm(x, axis=-1)
    return x + _A_ref(xn, A_wqkv, A_wo) + _B_ref(xn, B_w1, B_w2)
```

```cpp
#include <hip/hip_runtime.h>
#include <utility>

typedef unsigned u32;
typedef unsigned long long u64;
typedef u32 v4u __attribute__((ext_vector_type(4)));
typedef u32 v2u __attribute__((ext_vector_type(2)));
typedef float f4 __attribute__((ext_vector_type(4)));
typedef float f2 __attribute__((ext_vector_type(2)));
typedef short s4v __attribute__((ext_vector_type(4)));
typedef __bf16 bf2 __attribute__((ext_vector_type(2)));
typedef __bf16 bf8 __attribute__((ext_vector_type(8)));
typedef __amdgpu_buffer_rsrc_t rsrc_t;
#define DI __device__ __forceinline__
#define LAS __attribute__((address_space(3)))
template <int... Is> using iseq = std::integer_sequence<int, Is...>;
template <int N> using mkseq = std::make_integer_sequence<int, N>;

constexpr u32 WS_ARR = 4096, WS_ROW = 65536, WS_COL = 131072, WS_FLAG = 262144, WS_H = (2u << 20) + 4096;
constexpr u32 H_CI = 36864 + 128, H_RT = 8 * H_CI;
constexpr u32 WS_USED = WS_H + 32 * H_RT;
constexpr int LW = 5120;
constexpr int SLOT_F = 45056, SLOT_Q = 40960, HALF_OFF = 40960, HALO = 3 * SLOT_Q;
constexpr int TAB = 159744, T_MEAN = TAB, T_TOK = TAB, T_CP = TAB, T_CS = TAB + 2560, T_PAIR = TAB + 3456, T_DUMP = TAB + 3584;
constexpr u32 w2slot(int k, u32 w) {
  return k == 0 ? 90112 + 4096 * w : k == 1 ? (w < 3 ? 122880 : 126976) + 4096 * w : k == 2 ? 4096 * w : k == 3 ? 45056 + 4096 * w
       : (w < 3 ? 32768 + 4096 * w : w < 6 ? 65536 + 4096 * w : w == 6 ? 135168u : 90112 + 4096 * w);
}
constexpr int LDS_BYTES = 163840;
constexpr u32 SPIN_CAP = 1u << 18;
constexpr u32 OOB = 0x80000000u;

enum { RQ = 0, RF0 = 1, RF1 = 2 };
template <int R> struct RC {
  static constexpr int NA = R == RQ ? 5 : 4, NB = R == RF0 ? 4 : 3, NM = R == RQ ? 14 : (R == RF0 ? 16 : 12);
  static constexpr int SLOT = R == RQ ? SLOT_Q : SLOT_F, RS = R == RQ ? 3072 : 3584, NBASE = R == RQ ? 1 : (R == RF0 ? 4 : 3);
};

struct Params { const char *x, *wqkv, *wo, *w1, *w2; char *out, *ws; };

DI u32 rfl(u32 x) { return __builtin_amdgcn_readfirstlane(x); }
DI rsrc_t mkrsrc(const void* p, u32 bytes) { return __builtin_amdgcn_make_buffer_rsrc((void*)p, 0, bytes, 0x00020000); }
DI u32 cvtpk(float lo, float hi) { f2 v = {lo, hi}; return __builtin_bit_cast(u32, __builtin_convertvector(v, bf2)); }
DI float bflo(u32 v) { return __uint_as_float(v << 16); }
DI float bfhi(u32 v) { return __uint_as_float(v & 0xffff0000u); }
template <int CTRL> DI float dpp(float x) { return __builtin_bit_cast(float, __builtin_amdgcn_mov_dpp(__builtin_bit_cast(int, x), CTRL, 0xf, 0xf, true)); }
DI float sw16_sum(float x) { auto s = __builtin_amdgcn_permlane16_swap(__float_as_uint(x), __float_as_uint(x), false, false); return __uint_as_float(s[0]) + __uint_as_float(s[1]); }
DI float sw16_max(float x) { auto s = __builtin_amdgcn_permlane16_swap(__float_as_uint(x), __float_as_uint(x), false, false); return fmaxf(__uint_as_float(s[0]), __uint_as_float(s[1])); }
DI float sw32_max(float x) { auto s = __builtin_amdgcn_permlane32_swap(__float_as_uint(x), __float_as_uint(x), false, false); return fmaxf(__uint_as_float(s[0]), __uint_as_float(s[1])); }
template <class T> DI T ldl(u32 a) { return *(const LAS T*)(size_t)a; }
template <class T> DI void stl(u32 a, T v) { *(LAS T*)(size_t)a = v; }
DI f4 mfma_b(const v4u& a, const v4u& b, const f4& c) { return __builtin_amdgcn_mfma_f32_16x16x32_bf16(__builtin_bit_cast(bf8, a), __builtin_bit_cast(bf8, b), c, 0, 0, 0); }

template <int OFF> DI void rd128(v4u& d, u32 a) { asm volatile("ds_read_b128 %0, %1 offset:%2" : "=v"(d) : "v"(a), "i"(OFF) : "memory"); }
template <int OFF> DI void rdf4(f4& d, u32 a) { asm volatile("ds_read_b128 %0, %1 offset:%2" : "=v"(d) : "v"(a), "i"(OFF) : "memory"); }
template <int OFF> DI void rd32(float& d, u32 a) { asm volatile("ds_read_b32 %0, %1 offset:%2" : "=v"(d) : "v"(a), "i"(OFF) : "memory"); }
template <int OFF> DI void rdtr(v2u& d, u32 a) { asm volatile("ds_read_b64_tr_b16 %0, %1 offset:%2" : "=v"(d) : "v"(a), "i"(OFF) : "memory"); }
template <int LOFF> DI void m0_set(u32 base) { asm volatile("s_add_u32 m0, %0, %1" ::"s"(base), "i"(LOFF) : "memory", "scc"); }
template <int IMM> DI void dma_imm(u32 voff, rsrc_t r, u32 soff) {
  asm volatile("buffer_load_dwordx4 %0, %1, %2 offen offset:%3 lds" ::"v"(voff), "s"(r), "s"(soff), "i"(IMM) : "memory");
}
template <int LOFF> DI void dma(u32 m0base, u32 voff, rsrc_t r, u32 soff) {
  asm volatile("s_add_u32 m0, %0, %1\n\ts_nop 0\n\tbuffer_load_dwordx4 %2, %3, %4 offen lds" ::"s"(m0base), "i"(LOFF), "v"(voff), "s"(r), "s"(soff) : "memory", "scc");
}
template <int IMM> DI void gld(v4u& d, u32 voff, rsrc_t r, u32 soff) { asm volatile("buffer_load_dwordx4 %0, %1, %2, %3 offen offset:%4" : "=v"(d) : "v"(voff), "s"(r), "s"(soff), "i"(IMM) : "memory"); }
template <int IMM> DI void gld_sc1(v4u& d, u32 voff, rsrc_t r, u32 soff) { asm volatile("buffer_load_dwordx4 %0, %1, %2, %3 offen offset:%4 sc1" : "=v"(d) : "v"(voff), "s"(r), "s"(soff), "i"(IMM) : "memory"); }
template <int IMM> DI void gld_t(v4u& d, u32 voff, rsrc_t r) { asm volatile("buffer_load_dwordx4 %0, %1, %2, 0 offen offset:%3 sc1" : "+v"(d) : "v"(voff), "s"(r), "i"(IMM) : "memory"); }
template <int IMM> DI void gst_sc1(const v4u& d, u32 voff, rsrc_t r, u32 soff) { asm volatile("buffer_store_dwordx4 %0, %1, %2, %3 offen offset:%4 sc1\n\ts_nop 1" ::"v"(d), "v"(voff), "s"(r), "s"(soff), "i"(IMM) : "memory"); }
DI void gst1_sc1(u32 d, u32 voff, rsrc_t r) { asm volatile("buffer_store_dword %0, %1, %2, 0 offen sc1" ::"v"(d), "v"(voff), "s"(r) : "memory"); }
DI void touch(u32& d, u64 a, u64 lanes) {
  asm volatile("s_mov_b64 exec, %2\n\tglobal_load_dword %0, %1, off\n\ts_mov_b64 exec, -1" : "=v"(d) : "v"(a), "s"(lanes) : "memory");
}
template <int N> DI void wait_vm() { asm volatile("s_waitcnt vmcnt(%0)" ::"i"(N) : "memory"); }
template <int N> DI void wait_lgkm() { asm volatile("s_waitcnt lgkmcnt(%0)" ::"i"(N) : "memory"); }
template <int N, int L = 0> DI void wait_bar() { asm volatile("s_waitcnt vmcnt(%0) lgkmcnt(%1)\n\ts_barrier" ::"i"(N), "i"(L) : "memory"); }
DI void bar() { asm volatile("s_waitcnt lgkmcnt(0)\n\ts_barrier" ::: "memory"); }
template <bool FIRST> DI void mfma(f4& c, const v4u& a, const v4u& b) {
  if constexpr (FIRST) asm volatile("v_mfma_f32_16x16x32_bf16 %0, %1, %2, 0" : "=&v"(c) : "v"(a), "v"(b));
  else asm volatile("v_mfma_f32_16x16x32_bf16 %0, %1, %2, %0" : "+v"(c) : "v"(a), "v"(b));
}
DI v4u landed(const v4u& x) {
  u64 a, b;
  asm volatile("v_mov_b64 %0, %2\n\tv_mov_b64 %1, %3" : "=&v"(a), "=&v"(b) : "v"(__builtin_bit_cast(u64, (v2u){x.x, x.y})), "v"(__builtin_bit_cast(u64, (v2u){x.z, x.w})));
  const v2u p = __builtin_bit_cast(v2u, a), q = __builtin_bit_cast(v2u, b);
  return (v4u){p.x, p.y, q.x, q.y};
}
template <int N> DI u32 landed_scalar(u32 x) { u32 r; asm volatile("s_waitcnt vmcnt(%2)\n\tv_readfirstlane_b32 %0, %1\n\ts_nop 1" : "=s"(r) : "v"(x), "i"(N) : "memory"); return r; }
DI v4u cat(const v2u& l, const v2u& h) { return (v4u){l.x, l.y, h.x, h.y}; }
#define PIN(x) asm volatile("" : "+v"(x))
#define KEEP(x) asm volatile("" ::"v"(x))
#define SB() __builtin_amdgcn_sched_barrier(0)
template <int N, class F, int... Is> DI void sfor_(F&& f, iseq<Is...>) { (f(std::integral_constant<int, Is>{}), ...); }
template <int N, class F> DI void sfor(F&& f) { sfor_<N>(f, mkseq<N>{}); }

struct Ctx {
  u32 lane, g, i16, w, wg, rt, ci, rt2, ct2, tag, krot;
  bool retired;
  rsrc_t rx, rws, rw2, rwo;
  u32 vw2;
  u32 sl[5], slp[4];
  u32 aW[2];
  v2u wl[4][4], wh[4][4];
  v4u xr;
  u32 vo;
};
DI u32 colL0(u32 ci) { return 224 * ci + ((ci & 1) ? 32 : 0); }
DI u32 colHf(u32 ci) { return 224 * ci + ((ci & 1) ? 0 : 192); }
DI u32 tile_row(u32 T, u32 i) { return 64 * (T >> 2) + 32 * (T & 1) + 2 * i + ((T >> 1) & 1); }
DI int halo_row(u32 i) { return -16 + 2 * (int)(i & 7) + (int)(i >> 3); }

constexpr int hblk(int S) { return S == 0 ? 7 : S < 5 ? S - 1 : S == 5 ? 8 : S - 2; }
constexpr int slot_of(int S) { return S == 8 ? 4 : S & 3; }
template <int S, int O> DI void w2_m0(const Ctx& c) { m0_set<0>(O ? c.slp[slot_of(S) & 3] : c.sl[slot_of(S)]); }
template <int S, int D, int O> DI void w2_ld(const Ctx& c) {
  const u32 tw = O ? c.w ^ 4 : c.w;
  constexpr int B = hblk(S);
  if constexpr (B >= 7) dma_imm<D * 1024>(c.vw2, c.rwo, (64 * tw + 32 * (B - 7) + 7 * D) * 1024);
  else if constexpr (B < 6) dma_imm<D * 1024>(c.vw2, c.rw2, (colL0(tw) + 32 * B + 7 * D) * 1024);
  else dma_imm<D * 1024>(c.vw2, c.rw2, (colHf(tw) + 7 * D) * 1024);
}
template <int S, int O = 0> DI void dma_ks(const Ctx& c) {
  w2_m0<S, O>(c);
  asm volatile("s_nop 0");
  w2_ld<S, 0, O>(c); w2_ld<S, 1, O>(c); w2_ld<S, 2, O>(c); w2_ld<S, 3, O>(c);
}
template <int S, int I> DI void w2_read(Ctx& c) {
  constexpr int n = I >> 1, t = I & 1;
  rdtr<t * 2048 + (n & 1) * 8>(t ? c.wh[S & 3][n] : c.wl[S & 3][n], c.aW[n >> 1] + c.sl[slot_of(S)]);
}
template <int S> DI void w2_read_ks(Ctx& c) { sfor<8>([&](auto ic) { w2_read<S, ic>(c); }); }

struct TabF { f4 tok[4], cs[4]; };
struct TabQ { f4 csq, csk; float csv, mi[5], ri[5]; f4 mr[5], rr[5]; };
template <int R> struct S1 {
  TabF tf; TabQ tq;
  f4 acc[RC<R>::NM];
  v4u xa[2][RC<R>::NA];
  v2u wl[2][RC<R>::NB], wh[2][RC<R>::NB];
  u32 vd[6], vhalo[2];
  u32 aA[2], aH[2], aB[RC<R>::NBASE];
  u32 m0w, m0h, m0x;
  rsrc_t rw, rc;
  v4u rows[2], ext[4], gat[2], arr[2], sel;
  u32 vs[3], srs, vgat[2], aT[3], aTw[2];
  u64 wa;
  u32 wt, touch, epoch;
};

constexpr int PRE1 = 3;
constexpr int ST = 1, GT = 6, TT = 2;
constexpr int op_chunk(int ch, int vp, int d) { return 1000 + ch * 100 + vp * 10 + d; }
constexpr int op_w2(int s2, int d) { return 2000 + s2 * 10 + d; }
constexpr int op_halo(int hi, int d) { return 3000 + hi * 10 + d; }
constexpr int OP_MISC = 4000;
constexpr int chunk_piece(int ch, int i) { return i < 5 ? op_chunk(ch, 0, i) : i < 10 ? op_chunk(ch, 1, i - 5) : op_chunk(ch, 1, 5); }
struct OpList { int n; int op[16]; };
constexpr OpList phase_ops(int R, int G, int T, int PH) {
  OpList L{};
  const int NC = R == RQ ? 10 : 11;
  if (G == 1) {
    if (T == 0 && PH == 0) for (int i = 0; i < 6; ++i) L.op[L.n++] = chunk_piece(2, i);
    if (T == 0 && PH == 1) for (int i = 6; i < NC; ++i) L.op[L.n++] = chunk_piece(2, i);
    if (PH == 1 && T + 3 <= 7) for (int i = 0; i < 5; ++i) L.op[L.n++] = chunk_piece(T + 3, i);
    if (PH == 0 && T >= 1 && T + 2 <= 7) for (int i = 5; i < NC; ++i) L.op[L.n++] = chunk_piece(T + 2, i);
    const int q = 2 * T + PH - 11;
    const int lo[4] = {0, 6, 11, 16};
    if (q >= 0 && q < 3) for (int i = lo[q]; i < lo[q + 1]; ++i) L.op[L.n++] = op_w2(i >> 3, (i & 7) ^ 4);
    if (q >= 3) for (int d = 0; d < 4; ++d) L.op[L.n++] = op_w2(q - 1, d);
  }
  if (R == RQ && G == 0 && T == 1 && PH == 0) { L.op[L.n++] = op_halo(1, 0); L.op[L.n++] = op_halo(1, 1); }
  return L;
}
constexpr int tail_ops(int G, int T, int PH) {
  int n = 0;
  if (G == 0 && T == 0 && PH == 0) n += 1;
  if (G == 0 && T == TT && PH == 0) n += 1;
  if (G == 1 && T == ST && PH == 0) n += 4;
  if (G == 1 && T == ST + 2 && PH == 1) n += 1;
  if (G == 1 && T == GT && PH == 0) n += 2;
  if (T == 7 && PH == 0) n += 1;
  return n;
}
constexpr int younger(int R, int G, int T, int PH, bool TAIL, int lo, int hi) {
  int n = 0;
  if (G == 0) { for (int i = 0; i < PRE1; ++i) { const int o = op_chunk(1, 0, i); n = (o >= lo && o < hi) ? 0 : n + 1; } n += 2; }
  else {
    n += 2;
    for (int i = PRE1; i < (R == RQ ? 10 : 11); ++i) { const int o = chunk_piece(1, i); n = (o >= lo && o < hi) ? 0 : n + 1; }
  }
  for (int t = 0; t <= T; ++t)
    for (int ph = 0; ph < 2; ++ph) {
      if (t == T && ph > PH) break;
      const OpList L = phase_ops(R, G, t, ph);
      for (int i = 0; i < L.n; ++i) n = (L.op[i] >= lo && L.op[i] < hi) ? 0 : n + 1;
      if (t == T && ph == PH && !TAIL) break;
      const int id = OP_MISC + t * 2 + ph;
      for (int i = 0; i < tail_ops(G, t, ph); ++i) n = (id >= lo && id < hi) ? 0 : n + 1;
    }
  return n > 63 ? 63 : n;
}

constexpr int m0_key(int op) { return op >= 3000 ? op / 10 : op >= 2000 ? op / 10 * 2 + (op % 10) / 4 + 10000 : op / 10 * 4 + (op % 10 < 4 ? 0 : op % 10 - 3) + 20000; }
template <int R, int OP> DI void op_m0(const Ctx& c, S1<R>& s) {
  if constexpr (OP >= 3000) m0_set<HALO + (OP % 100 / 10) * 8192>(s.m0x);
  else if constexpr (OP >= 2000) w2_m0<(OP % 1000) / 10, (OP % 10) / 4>(c);
  else {
    constexpr int CH = (OP % 1000) / 100, VP = OP % 100 / 10, D = OP % 10, so = (CH % 3) * RC<R>::SLOT;
    if constexpr (D < 5) m0_set<so + VP * 4 * LW + (D == 4 ? 4096 : 0)>(s.m0w); else m0_set<so>(s.m0h);
  }
}
template <int R, int OP> DI void op_ld(const Ctx& c, S1<R>& s) {
  if constexpr (OP >= 3000) dma_imm<(OP % 10) * 1024>(s.vhalo[OP % 10], c.rx, (((c.w & 3) + (OP % 100 / 10) * 4 + c.krot) & 7) * 128);
  else if constexpr (OP >= 2000) w2_ld<(OP % 1000) / 10, (OP % 10) & 3, (OP % 10) / 4>(c);
  else {
    constexpr int CH = (OP % 1000) / 100, VP = OP % 100 / 10, D = OP % 10, RS = RC<R>::RS;
    const u32 kc = (CH + c.krot) & 7;
    if constexpr (D < 2) dma_imm<D * 1024>(s.vd[D], c.rx, kc * 128 + VP * 65536);
    else if constexpr (D < 5) dma_imm<(D & 3) * 1024>(s.vd[D], s.rw, (kc * 64 + VP * 32) * RS);
    else dma_imm<0>(s.vd[5], s.rw, kc * 64 * RS);
  }
}
template <int R, int OP> DI void do_op(const Ctx& c, S1<R>& s) { op_m0<R, OP>(c, s); asm volatile("s_nop 0"); op_ld<R, OP>(c, s); }

template <int R, int CH, int KS, int BUF, int I> DI void frag_read(S1<R>& s) {
  constexpr int NA = RC<R>::NA, so = (CH % 3) * RC<R>::SLOT;
  constexpr int ord = I == 0 ? 0 : (I < 3 ? 100 + (I - 1) : (I < NA + 2 ? I - 2 : 100 + (I - NA)));
  if constexpr (ord < 100) {
    if constexpr (R == RQ && ord == 4) rd128<CH * 2048>(s.xa[BUF][4], s.aH[KS]);
    else if constexpr (R == RQ) rd128<((ord >> 1) * 4 + (ord & 1)) * LW>(s.xa[BUF][ord], s.aA[KS] + so);
    else rd128<ord * LW>(s.xa[BUF][ord], s.aA[KS] + so);
  } else {
    constexpr int n = (ord - 100) >> 1, t = (ord - 100) & 1;
    v2u& d = t ? s.wh[BUF][n] : s.wl[BUF][n];
    if constexpr (R == RQ) rdtr<KS * 4 * LW + n * 1024 + t * 512>(d, s.aB[0] + so);
    else if constexpr (R == RF1 && n == 2) rdtr<KS * 2048 + t * 256>(d, s.aB[2] + so);
    else rdtr<KS * 4 * LW + t * 512>(d, s.aB[n] + so);
  }
}
template <int R, int BUF, bool FIRST, int M> DI void s1_mfma(S1<R>& s) {
  if constexpr (R == RQ) {
    if constexpr (M < 4) mfma<FIRST>(s.acc[M], cat(s.wl[BUF][0], s.wh[BUF][0]), s.xa[BUF][M]);
    else if constexpr (M < 9) mfma<FIRST>(s.acc[M], cat(s.wl[BUF][1], s.wh[BUF][1]), s.xa[BUF][M - 4]);
    else mfma<FIRST>(s.acc[M], s.xa[BUF][M - 9], cat(s.wl[BUF][2], s.wh[BUF][2]));
  } else mfma<FIRST>(s.acc[M], cat(s.wl[BUF][M >> 2], s.wh[BUF][M >> 2]), s.xa[BUF][M & 3]);
}
template <int R, int I> DI void tab_read(S1<R>& s) {
  if constexpr (R == RQ) {
    constexpr int j = I >> 2, k = I & 3, o = (j < 4 ? (j >> 1) * 4 + (j & 1) : 8) * 64;
    if constexpr (I < 16) {
      if constexpr (k == 0) rd32<o>(s.tq.mi[j], s.aT[0]); else if constexpr (k == 1) rd32<640 + o>(s.tq.ri[j], s.aT[0]);
      else if constexpr (k == 2) rdf4<o>(s.tq.mr[j], s.aT[1]); else rdf4<640 + o>(s.tq.rr[j], s.aT[1]);
    } else if constexpr (I < 20) {
      if constexpr (k == 0) rd32<o>(s.tq.mi[4], s.aH[0]); else if constexpr (k == 1) rd32<640 + o>(s.tq.ri[4], s.aH[0]);
      else if constexpr (k == 2) rdf4<o>(s.tq.mr[4], s.aH[1]); else rdf4<640 + o>(s.tq.rr[4], s.aH[1]);
    } else if constexpr (I == 20) rdf4<0>(s.tq.csq, s.aT[2]);
    else if constexpr (I == 21) rdf4<256>(s.tq.csk, s.aT[2]);
    else if constexpr (I == 22) rd32<512>(s.tq.csv, s.aB[0]);
  } else {
    if constexpr (I < 4) rdf4<I * 256>(s.tf.tok[I], s.aT[0]);
    else if constexpr (I < 4 + RC<R>::NB) {
      constexpr int n = I - 4;
      if constexpr (R == RF1 && n == 2) rdf4<0>(s.tf.cs[2], s.aT[2]); else rdf4<n * 64>(s.tf.cs[n], s.aT[1]);
    }
  }
}
template <int R> DI void tab_addr(const Ctx& c, S1<R>& s) {
  const u32 g4 = c.g * 16, i4 = c.i16 * 4;
  if constexpr (R == RQ) {
    const u32 p = c.w & 1, hl = c.w >> 1;
    s.aT[0] = T_MEAN + p * 128 + i4; s.aT[1] = T_MEAN + p * 128 + g4; s.aT[2] = T_CS + hl * 64 + g4;
    s.aH[0] = T_MEAN + i4; s.aH[1] = T_MEAN + g4; s.aB[0] = T_CS + hl * 64 + i4;
  } else {
    const u32 wm = (c.w >> 1) & 1, wn = c.w & 1;
    s.aT[0] = T_TOK + wm * 1024 + i4 * 4;
    s.aT[1] = T_CS + (R == RF0 ? wn * 256 : 512 + wn * 128) + g4; s.aT[2] = T_CS + 768 + wn * 64 + g4;
  }
}
template <int R, int G, int T, int PH, int M> DI void gap(Ctx& c, S1<R>& s) {
  constexpr int NR = RC<R>::NA + 2 * RC<R>::NB, NM = RC<R>::NM;
  constexpr OpList L = phase_ops(R, G, T, PH);
  static_assert(L.n <= NM, "one load per gap at most");
  constexpr int k0 = (M * L.n + NM - 1) / NM, k1 = ((M + 1) * L.n + NM - 1) / NM;
  if constexpr (k1 > k0) { if constexpr (k0 == 0 || m0_key(L.op[k0]) != m0_key(L.op[k0 > 0 ? k0 - 1 : 0])) op_m0<R, L.op[k0]>(c, s); }
  s1_mfma<R, PH, T == 0 && PH == 0, M>(s);
  if constexpr (M < NR) {
    if constexpr (PH == 0) frag_read<R, T, 1, 1, M>(s);
    else if constexpr (T < 7) frag_read<R, T + 1, 0, 0, M>(s);
  }
  if constexpr (T == 7 && PH == 1) {
    if constexpr (M == 0) tab_addr<R>(c, s);
    if constexpr (R == RQ) { tab_read<R, 2 * M>(s); tab_read<R, 2 * M + 1>(s); } else tab_read<R, M>(s);
  }
  if constexpr (k1 > k0) op_ld<R, L.op[k0]>(c, s);
}
template <int R, int G, int T, int PH, int... Ms> DI void phase(Ctx& c, S1<R>& s, iseq<Ms...>) { (gap<R, G, T, PH, Ms>(c, s), ...); }

DI u32 stat_row(const Ctx& c) { return (c.wg & 7) * 256 + (c.wg >> 3) * 8 + 2 * (c.w & 3); }
DI u32 stat_blk(const Ctx& c) {
  const u32 x = c.wg & 7, j = c.wg >> 3, lb = j < 26 ? j : j - 26, b = x & 3;
  const u32 cb = lb < 12 ? (lb >> 2) * 16 + 4 * b + (lb & 3) : 48 + 14 * b + (lb - 12);
  return cb * 2 + (x >> 2);
}
DI void stats_rows(const Ctx& c, const v4u (&x)[2]) {
  const float xa = __uint_as_float(__builtin_amdgcn_readlane(x[0][0], 0) << 16), xb = __uint_as_float(__builtin_amdgcn_readlane(x[0][0], 32) << 16);
  const float x0 = c.lane < 32 ? xa : xb;
  f2 x00 = {x0, x0}, s1 = {0.f, 0.f}, s2 = {0.f, 0.f};
  PIN(x00);
#pragma unroll
  for (int e = 0; e < 8; ++e) {
    const f2 d = (f2){bflo(x[e >> 2][e & 3]), bfhi(x[e >> 2][e & 3])} - x00;
    s1 += d; s2 = __builtin_elementwise_fma(d, d, s2);
  }
  float r0 = s1.x + s1.y, r1 = s2.x + s2.y;
  r0 += dpp<0xB1>(r0); r1 += dpp<0xB1>(r1); r0 += dpp<0x4E>(r0); r1 += dpp<0x4E>(r1);
  r0 += dpp<0x141>(r0); r1 += dpp<0x141>(r1); r0 += dpp<0x128>(r0); r1 += dpp<0x128>(r1);
  r0 = sw16_sum(r0); r1 = sw16_sum(r1);
  const float m1 = r0 * (1.f / 512.f), var = fmaxf(fmaf(-m1, m1, r1 * (1.f / 512.f)), 0.f);
  const v4u gr = {__float_as_uint(m1 + x0), c.tag, __float_as_uint(__builtin_amdgcn_rsqf(var + 1e-5f)), c.tag};
  gst_sc1<0>(gr, (c.lane & 31) == 0 ? WS_ROW + (stat_row(c) + (c.lane >> 5)) * 16 : OOB, c.rws, 0);
}
template <int R> DI void cols_setup(const Ctx& c, S1<R>& s, const Params& p) {
  const u32 l = c.lane, w = c.w & 3, b = stat_blk(c), cb = b >> 1;
  const bool isq = cb < 48;
  s.srs = isq ? 3072 : 3584;
  s.rc = mkrsrc(isq ? p.wqkv : p.w1, 512 * s.srs);
  s.vs[1] = ((b & 1) * 256 + w * 64 + (l >> 2)) * s.srs + (isq ? cb : cb - 48) * 64 + (l & 3) * 16;
  s.vs[2] = l < 8 ? WS_COL + (cb * 32 + w * 8 + l) * 16 + (b & 1) * 8 : (l == 8 && w == 0) ? WS_ARR + c.wg * 8 : OOB;
#pragma unroll
  for (int e = 0; e < 4; ++e) s.sel[e] = (c.i16 >> 1) == (u32)e ? ((c.i16 & 1) ? 0x3f800000u : 0x00003f80u) : 0u;
  s.vs[0] = (c.i16 < 4 && c.g < 2) ? T_CP + w * 128 + (c.i16 * 8 + c.g * 4) * 4 : T_CP + 512 + (l & 31) * 16;
  PIN(s.vs[0]); PIN(s.vs[1]); PIN(s.vs[2]); PIN(s.sel);
}
template <int R> DI void cols_load(S1<R>& s) {
  gld<0>(s.ext[0], s.vs[1], s.rc, 0); gld<0>(s.ext[1], s.vs[1], s.rc, 16 * s.srs); gld<0>(s.ext[2], s.vs[1], s.rc, 32 * s.srs); gld<0>(s.ext[3], s.vs[1], s.rc, 48 * s.srs);
}
template <int R, int N> DI void cols_wave(S1<R>& s) {
  asm volatile("s_waitcnt vmcnt(%4)" : "+v"(s.ext[0]), "+v"(s.ext[1]), "+v"(s.ext[2]), "+v"(s.ext[3]) : "i"(N) : "memory");
  const f4 z = {0.f, 0.f, 0.f, 0.f};
  f4 ca = mfma_b(s.sel, s.ext[0], z), cb = mfma_b(s.sel, s.ext[1], z);
  ca = mfma_b(s.sel, s.ext[2], ca); cb = mfma_b(s.sel, s.ext[3], cb);
  ca += cb;
#pragma unroll
  for (int k = 0; k < 4; ++k) ca[k] += dpp<0x124>(ca[k]);
#pragma unroll
  for (int k = 0; k < 4; ++k) ca[k] += dpp<0x128>(ca[k]);
  asm volatile("ds_write_b128 %0, %1" ::"v"(s.vs[0]), "v"(ca) : "memory");
}
template <int R> DI void cols_out(const Ctx& c, S1<R>& s) {
  float v = ldl<float>(T_CP + ((c.lane >> 3) & 3) * 128 + ((c.w & 3) * 8 + (c.lane & 7)) * 4);
  v += dpp<0x128>(v); v = sw16_sum(v);
  const v2u gr = {__float_as_uint(v), c.tag};
  asm volatile("buffer_store_dwordx2 %0, %1, %2, 0 offen sc1" ::"v"(gr), "v"(s.vs[2]), "s"(c.rws) : "memory");
}
template <int R, int N> DI void tables(const Ctx& c, S1<R>& s) {
  constexpr u32 NC = R == RQ ? 48 : 56;
  asm volatile("s_waitcnt vmcnt(%2)" : "+v"(s.gat[0]), "+v"(s.gat[1]) : "i"(N) : "memory");
#pragma unroll 1
  for (u32 spin = 0; spin < SPIN_CAP; ++spin) {
    const u64 okc = __builtin_amdgcn_ballot_w64(s.gat[0].y == c.tag && s.gat[0].w == c.tag), okr = __builtin_amdgcn_ballot_w64(s.gat[1].y == c.tag && s.gat[1].w == c.tag);
    if ((~okc & ((1ull << NC) - 1)) == 0 && (~okr & ((1ull << 36) - 1)) == 0) break;
    __builtin_amdgcn_s_sleep(2);
    asm volatile("buffer_load_dwordx4 %0, %2, %4, 0 offen sc1\n\tbuffer_load_dwordx4 %1, %3, %4, 0 offen sc1\n\ts_waitcnt vmcnt(0)"
                 : "=&v"(s.gat[0]), "=&v"(s.gat[1]) : "v"(s.vgat[0]), "v"(s.vgat[1]), "s"(c.rws) : "memory");
  }
  const float a = __uint_as_float(s.gat[0].x) + __uint_as_float(s.gat[0].z), b = __uint_as_float(s.gat[1].z);
  asm volatile("ds_write_b32 %0, %1" ::"v"(s.aTw[0]), "v"(a) : "memory");
  if constexpr (R == RQ) asm volatile("ds_write2_b32 %0, %1, %2 offset1:160" ::"v"(s.aTw[1]), "v"(s.gat[1].x), "v"(b) : "memory");
  else {
    f4 t = {__uint_as_float(s.gat[1].x), b * -1.4426950408889634f, __builtin_amdgcn_rcpf(b), 0.f};
    asm volatile("s_nop 0\n\tds_write_b128 %0, %1" ::"v"(s.aTw[1]), "v"(t) : "memory");
  }
}
DI bool retire(const Ctx& c, const v4u (&a)[2]) {
  if (!__all(a[0].y == c.tag && a[0].w == c.tag && a[1].y == c.tag && a[1].w == c.tag)) return false;
  gst1_sc1(c.tag, c.lane == 0 ? 0u : OOB, c.rws);
  return true;
}

template <int R, int G, int T> DI void tick(Ctx& c, S1<R>& s) {
  constexpr int NM = RC<R>::NM;
  phase<R, G, T, 0>(c, s, mkseq<NM>{});
  SB();
  if constexpr (G == 1 && T == ST) cols_load<R>(s);
  if constexpr (G == 1 && T == ST + 1) cols_wave<R, younger(R, G, ST + 1, 0, false, OP_MISC + 2 * ST, OP_MISC + 2 * ST + 1)>(s);
  if constexpr (G == 1 && T == GT) {
    if (c.wg == 0 && c.w == 7) { gld_t<0>(s.arr[0], WS_ARR + c.lane * 32, c.rws); gld_t<16>(s.arr[1], WS_ARR + c.lane * 32, c.rws); }
    gld_sc1<0>(s.gat[0], s.vgat[0], c.rws, 0); gld_sc1<0>(s.gat[1], s.vgat[1], c.rws, 0);
  }
  if constexpr (G == 1 && T == GT + 1) {
    __builtin_amdgcn_s_setprio(0);
    tables<R, younger(R, G, GT + 1, 0, false, OP_MISC + 2 * GT, OP_MISC + 2 * GT + 1)>(c, s);
    PIN(s.arr[0]); PIN(s.arr[1]);
    if (c.wg == 0 && c.w == 7) c.retired = retire(c, s.arr);
    __builtin_amdgcn_s_setprio(1);
  }
  if constexpr (G == 0 && T == 0) { c.tag = landed_scalar<1>(s.epoch) + 1; stats_rows(c, s.rows); }
  if constexpr (G == 0 && T == TT) touch(s.touch, s.wa, 0x01ff01ff);
  if constexpr (T == 7) gld<0>(c.xr, c.vo, c.rx, 0);
  SB();
  constexpr int n = T == 7 ? younger(R, G, T, 0, true, op_w2(1, 0), op_w2(2, 0)) : (T == 3 && G == 0) ? 1 : younger(R, G, T, 0, true, op_chunk(T + 1, 0, 0), op_chunk(T + 2, 0, 0));
  static_assert(TT >= 1 && TT <= 3 && GT == 6, "");
  wait_bar<n, (G == 1 && T == ST + 1) ? 1 : 0>();
  if constexpr (T == (G == 0 ? 3 : 0)) KEEP(s.wt);
  if constexpr (G == 1 && T == 0) c.tag = landed_scalar<63>(s.epoch) + 1;
  phase<R, G, T, 1>(c, s, mkseq<NM>{});
  if constexpr (G == 1 && T == ST + 2) { SB(); cols_out<R>(c, s); SB(); }
  wait_lgkm<0>();
}
template <int R, int G, int... Ts> DI void ticks(Ctx& c, S1<R>& s, iseq<Ts...>) { (tick<R, G, Ts>(c, s), ...); }

template <int G> DI void epi_head(Ctx& c) {
  if constexpr (G == 1) {
    __builtin_amdgcn_s_setprio(0);
    w2_read_ks<0>(c); w2_read_ks<1>(c);
    dma_ks<2, 1>(c); dma_ks<3, 1>(c);
    wait_lgkm<0>();
    dma_ks<4>(c); dma_ks<5>(c);
    wait_vm<8>();
    stl<u32>(T_PAIR + (c.w & 3) * 4, 1u);
  }
}
DI void publish(const Ctx& c) {
  gst1_sc1(c.tag, c.lane == 0 ? WS_FLAG + ((c.rt * 16 + c.ci * 2 + ((c.wg >> 3) & 1)) * 8 + c.w) * 128 : OOB, c.rws);
}
template <int G> DI void epi_tail(Ctx& c) {
  if constexpr (G == 0) {
    w2_read_ks<0>(c); w2_read_ks<1>(c);
    wait_lgkm<0>();
    dma_ks<4>(c); dma_ks<5>(c);
    wait_vm<8>();
    publish(c);
#pragma unroll 1
    for (u32 spin = 0; spin < SPIN_CAP; ++spin) {
      u32 f;
      asm volatile("ds_read_b32 %0, %1\n\ts_waitcnt lgkmcnt(0)" : "=&v"(f) : "v"(T_PAIR + (c.w & 3) * 4) : "memory");
      if (rfl(f)) break;
      __builtin_amdgcn_s_sleep(1);
    }
  }
  w2_read_ks<2>(c); w2_read_ks<3>(c);
  wait_lgkm<0>();
  if constexpr (G == 0) { dma_ks<6>(c); dma_ks<7>(c); dma_ks<8>(c); }
}
template <int R> DI void epi_ffn(Ctx& c, S1<R>& s, const TabF& t) {
  constexpr int NB = RC<R>::NB;
  const u32 wm = (c.w >> 1) & 1, wn = c.w & 1;
  const u32 vh0 = WS_H + (c.rt * 2 + wm) * H_RT + c.ci * H_CI + c.lane * 16;
  const u32 vh = vh0 + wn * (R == RF0 ? 8192 : 4096), vh2 = vh0 + wn * 2048;
  f2 c1p[4];
#pragma unroll
  for (int m = 0; m < 4; ++m) {
    c1p[m] = (f2){t.tok[m][1], t.tok[m][1]};
    PIN(c1p[m]);
  }
  v2u pk[NB][4];
  sfor<NB>([&](auto ic) {
    constexpr int n = ic;
    SB();
    if constexpr (n > 0) PIN(pk[n - 1][3]);
    f2 u[8], e[8];
#pragma unroll
    for (int q = 0; q < 8; ++q) {
      const int m = q >> 1, hf = q & 1;
      u[q] = __builtin_elementwise_fma((f2){-t.tok[m][0], -t.tok[m][0]}, (f2){t.cs[n][2 * hf], t.cs[n][2 * hf + 1]}, (f2){s.acc[n * 4 + m][2 * hf], s.acc[n * 4 + m][2 * hf + 1]});
    }
#pragma unroll
    for (int q = 0; q < 8; ++q) e[q] = u[q] * c1p[q >> 1];
    SB();
#pragma unroll
    for (int q = 0; q < 8; ++q) e[q] = (f2){__builtin_amdgcn_exp2f(e[q].x), __builtin_amdgcn_exp2f(e[q].y)};
    SB();
#pragma unroll
    for (int q = 0; q < 8; ++q) e[q] = __builtin_elementwise_fma(e[q], (f2){t.tok[q >> 1][2], t.tok[q >> 1][2]}, (f2){t.tok[q >> 1][2], t.tok[q >> 1][2]});
    SB();
#pragma unroll
    for (int q = 0; q < 8; ++q) e[q] = (f2){__builtin_amdgcn_rcpf(e[q].x), __builtin_amdgcn_rcpf(e[q].y)};
    SB();
#pragma unroll
    for (int q = 0; q < 8; ++q) u[q] *= e[q];
#pragma unroll
    for (int m = 0; m < 4; ++m) pk[n][m] = (v2u){cvtpk(u[2 * m].x, u[2 * m].y), cvtpk(u[2 * m + 1].x, u[2 * m + 1].y)};
    if constexpr (n & 1) {
      constexpr int so = (R == RF0 ? (n >> 1) : 4) * 4096;
      gst_sc1<0>(cat(pk[n - 1][0], pk[n][0]), vh, c.rws, so); gst_sc1<1024>(cat(pk[n - 1][1], pk[n][1]), vh, c.rws, so);
      gst_sc1<2048>(cat(pk[n - 1][2], pk[n][2]), vh, c.rws, so); gst_sc1<3072>(cat(pk[n - 1][3], pk[n][3]), vh, c.rws, so);
    } else if constexpr (n == NB - 1) {
      gst_sc1<0>(cat(pk[n][0], pk[n][1]), vh2, c.rws, 6 * 4096); gst_sc1<1024>(cat(pk[n][2], pk[n][3]), vh2, c.rws, 6 * 4096);
    }
  });
}
DI void epi_qkv(Ctx& c, S1<RQ>& s, const TabQ& tb) {
  const u32 hl = c.w >> 1, p = c.w & 1, g = c.g, i = c.i16;
  const float NEG = -1.0e30f;
  f4 bc, bp, bh;
  u32 mc_[2] = {0, 0}, mp_[2] = {0, 0}, mh_[2] = {0, 0};
#pragma unroll
  for (int r = 0; r < 4; ++r) {
    const int kk = 4 * g + r, kh = kk - 8 * (int)p;
    const bool vc = (u32)((int)i - kk) < 8u, vp = kk >= (int)i + 9, vh = (u32)kh < 8u && kh >= (int)i + 1 && c.rt > 0;
    const u32 bit = (r & 1) ? 0xffff0000u : 0x0000ffffu;
    bc[r] = vc ? 0.f : NEG; bp[r] = vp ? 0.f : NEG; bh[r] = vh ? 0.f : NEG;
    mc_[r >> 1] |= vc ? bit : 0u; mp_[r >> 1] |= vp ? bit : 0u; mh_[r >> 1] |= vh ? bit : 0u;
  }
  v2u q[4], k[5], v[5];
#pragma unroll
  for (int j = 0; j < 5; ++j) {
    const f2 mi = {-tb.mi[j], -tb.mi[j]}, ri = {tb.ri[j], tb.ri[j]};
    u32 o[2];
    if (j < 4) {
#pragma unroll
      for (int h = 0; h < 2; ++h) { const f2 t = __builtin_elementwise_fma(mi, (f2){tb.csq[2 * h], tb.csq[2 * h + 1]}, (f2){s.acc[j][2 * h], s.acc[j][2 * h + 1]}); o[h] = cvtpk(t.x, t.y); }
      q[j] = (v2u){o[0], o[1]};
    }
#pragma unroll
    for (int h = 0; h < 2; ++h) { const f2 t = ri * __builtin_elementwise_fma(mi, (f2){tb.csk[2 * h], tb.csk[2 * h + 1]}, (f2){s.acc[4 + j][2 * h], s.acc[4 + j][2 * h + 1]}); o[h] = cvtpk(t.x, t.y); }
    k[j] = (v2u){o[0], o[1]};
  }
  f4 sp[4], sc[4];
#pragma unroll
  for (int j = 0; j < 4; ++j) {
    sp[j] = __builtin_amdgcn_mfma_f32_16x16x16bf16_1k(__builtin_bit_cast(s4v, k[j == 0 ? 4 : j - 1]), __builtin_bit_cast(s4v, q[j]), j == 0 ? bh : bp, 0, 0, 0);
    sc[j] = __builtin_amdgcn_mfma_f32_16x16x16bf16_1k(__builtin_bit_cast(s4v, k[j]), __builtin_bit_cast(s4v, q[j]), bc, 0, 0, 0);
  }
  SB();
#pragma unroll
  for (int j = 0; j < 5; ++j) {
    const f2 cv = {tb.csv, tb.csv};
    u32 o[2];
#pragma unroll
    for (int h = 0; h < 2; ++h) {
      const f2 t = (f2){tb.rr[j][2 * h], tb.rr[j][2 * h + 1]} * __builtin_elementwise_fma((f2){-tb.mr[j][2 * h], -tb.mr[j][2 * h + 1]}, cv, (f2){s.acc[9 + j][2 * h], s.acc[9 + j][2 * h + 1]});
      o[h] = cvtpk(t.x, t.y);
    }
    v[j] = (v2u){o[0], o[1]};
  }
  float m[4];
#pragma unroll
  for (int j = 1; j < 4; ++j)
#pragma unroll
    for (int r = 0; r < 4; ++r) sc[j][r] = fmaxf(sp[j][r], sc[j][r]);
#pragma unroll
  for (int j = 0; j < 4; ++j) m[j] = fmaxf(fmaxf(sc[j][0], sc[j][1]), fmaxf(sc[j][2], sc[j][3]));
  m[0] = fmaxf(m[0], fmaxf(fmaxf(sp[0][0], sp[0][1]), fmaxf(sp[0][2], sp[0][3])));
#pragma unroll
  for (int j = 0; j < 4; ++j) m[j] = sw16_max(m[j]);
#pragma unroll
  for (int j = 0; j < 4; ++j) m[j] = sw32_max(m[j]);
  f2 z[10];
#pragma unroll
  for (int j = 0; j < 5; ++j) {
    const int jj = j & 3;
    const float c2 = tb.ri[jj] * (0.25f * 1.4426950408889634f), mc = -m[jj] * c2;
    const f4 sv = j < 4 ? sc[jj] : sp[0];
    z[2 * j] = __builtin_elementwise_fma((f2){sv[0], sv[1]}, (f2){c2, c2}, (f2){mc, mc});
    z[2 * j + 1] = __builtin_elementwise_fma((f2){sv[2], sv[3]}, (f2){c2, c2}, (f2){mc, mc});
  }
  SB();
  u32 e[10];
#pragma unroll
  for (int j = 0; j < 10; ++j) z[j] = (f2){__builtin_amdgcn_exp2f(z[j].x), __builtin_amdgcn_exp2f(z[j].y)};
#pragma unroll
  for (int j = 0; j < 10; ++j) e[j] = cvtpk(z[j].x, z[j].y);
  const f4 zero = {0.f, 0.f, 0.f, 0.f};
  const v4u ones = {0x3f803f80u, 0x3f803f80u, 0x3f803f80u, 0x3f803f80u};
  f4 ot[4], ls[4];
#pragma unroll
  for (int j = 0; j < 4; ++j) {
    const v4u pf = j == 0 ? (v4u){e[8] & mh_[0], e[9] & mh_[1], e[0] & mc_[0], e[1] & mc_[1]}
                          : (v4u){e[2 * j] & mp_[0], e[2 * j + 1] & mp_[1], e[2 * j] & mc_[0], e[2 * j + 1] & mc_[1]};
    ls[j] = mfma_b(ones, pf, zero);
    ot[j] = mfma_b(cat(v[j == 0 ? 4 : j - 1], v[j]), pf, zero);
  }
  const u32 vo = WS_H + c.rt * 2 * H_RT + c.ci * H_CI + (7 + (hl >> 1)) * 4096 + (hl & 1) * 2048 + p * 1024 + c.lane * 16;
  v2u o[4];
#pragma unroll
  for (int j = 0; j < 4; ++j) {
    const float il = __builtin_amdgcn_rcpf(ls[j][0]);
    const f2 a = (f2){ot[j][0], ot[j][1]} * (f2){il, il}, b = (f2){ot[j][2], ot[j][3]} * (f2){il, il};
    o[j] = (v2u){cvtpk(a.x, a.y), cvtpk(b.x, b.y)};
  }
  gst_sc1<0>(cat(o[0], o[1]), vo, c.rws, 0);
  gst_sc1<0>(cat(o[2], o[3]), vo, c.rws, H_RT);
}

struct Pro { u32 vd[6], vhalo[2], ctouch; u64 wa; v4u rows[2]; };
DI void prologue(Ctx& c, const Params& p, Pro& s, bool isq) {
  const u32 l = c.lane, v = c.w & 3, G = c.w >> 2, r0 = c.rt * 128, rs = isq ? 3072 : 3584, m0 = c.w * LW, kc = c.krot;
#pragma unroll
  for (int d = 0; d < 2; ++d) { const u32 ii = 8 * d + (l >> 3); s.vd[d] = (r0 + tile_row(v, ii)) * 1024 + (((l & 7) ^ ((ii >> 1) & 7)) << 4); }
  s.vd[1] -= 1024;
  m0_set<0>(m0);
  asm volatile("s_nop 0");
  dma_imm<0>(s.vd[0], c.rx, kc * 128 + G * 65536); dma_imm<1024>(s.vd[1], c.rx, kc * 128 + G * 65536);
  const u32 rho = l >> 3, s16 = l & 7, unit = (s16 >> 1) ^ (((rho >> 1) & 1) | ((v & 1) << 1));
  const u32 ls = isq ? 1024 : 128, b = (8 * v + rho) * rs + unit * 32 + (s16 & 1) * 16 + (isq ? c.ci * 128 : colL0(c.ci) * 2);
  const rsrc_t rw = mkrsrc((isq ? p.wqkv : p.w1) - 4096, 512 * rs + 4096);
  s.vd[2] = b + 2048; s.vd[3] = b + ls + 1024; s.vd[4] = b + 2 * ls + 4096;
  const u32 so = (kc * 64 + G * 32) * rs;
  dma_imm<2048>(s.vd[2], rw, so); dma_imm<3072>(s.vd[3], rw, so); dma<4096>(m0, s.vd[4], rw, so);
  s.vd[5] = s.vhalo[0] = s.vhalo[1] = 0;
  if (!isq && G) {
    const u32 rh = l >> 2, s4 = l & 3;
    s.vd[5] = (16 * v + rh) * 3584 + colHf(c.ci) * 2 + (((s4 >> 1) ^ ((rh >> 3) & 1)) << 5) + (s4 & 1) * 16 + 4096;
    dma<HALF_OFF>(v * 1024, s.vd[5], rw, kc * 64 * 3584);
  }
  if (isq && !G) {
#pragma unroll
    for (int d = 0; d < 2; ++d) {
      const u32 ii = 8 * d + (l >> 3);
      const int row = (int)r0 + halo_row(ii);
      s.vhalo[d] = row < 0 ? OOB : (u32)(row - d) * 1024 + (((l & 7) ^ ((ii >> 1) & 7)) << 4);
    }
    dma<HALO>(v * 2048, s.vhalo[0], c.rx, ((v + kc) & 7) * 128); dma_imm<1024>(s.vhalo[1], c.rx, ((v + kc) & 7) * 128);
  }
  {
    const u32 q = l & 15, wa = c.wg * 8 + c.w + (l >> 4) * 4;
    s.wa = q < 7 ? (u64)p.w2 + (wa * 7 + q) * 128 : q < 9 ? (u64)p.wo + (wa * 2 + q - 7) * 128 : (u64)p.x;
    PIN(s.wa);
  }
  asm volatile("s_barrier" ::: "memory");
  s.rows[0] = s.rows[1] = (v4u){0, 0, 0, 0};
  if (!G) {
    const u32 vr = (stat_row(c) + (l >> 5)) * 1024 + (l & 31) * 32, k1 = (kc + 1) & 7, m1 = m0 + (isq ? SLOT_Q : SLOT_F);
    gld<0>(s.rows[0], vr, c.rx, 0); gld<16>(s.rows[1], vr, c.rx, 0);
    static_assert(PRE1 == 3, "");
    dma<0>(m1, s.vd[0], c.rx, k1 * 128); dma_imm<1024>(s.vd[1], c.rx, k1 * 128); dma_imm<2048>(s.vd[2], rw, k1 * 64 * rs);
  }
}

template <int R, int G> DI void stage1(Ctx& c, const Params& p, Pro& pro) {
  S1<R> s;
  const u32 l = c.lane, w = c.w, v = w & 3, g = c.g, i = c.i16, q4 = i >> 2, p4 = i & 3;
  for (int d = 0; d < 6; ++d) s.vd[d] = pro.vd[d];
  s.vhalo[0] = pro.vhalo[0]; s.vhalo[1] = pro.vhalo[1];
  s.rw = mkrsrc((R == RQ ? p.wqkv : p.w1) - 4096, 512 * RC<R>::RS + 4096);
  s.m0w = v * LW; s.m0h = HALF_OFF + v * 1024; s.m0x = v * 2048;
  c.vw2 = (l >> 3) * 1024 + c.ct2 * 128 + (((l & 7) ^ (((l >> 4) & 1) << 2)) << 4);
#pragma unroll
  for (int k = 0; k < 4; ++k) { c.sl[k] = w2slot(k, w); c.slp[k] = w2slot(k, w ^ 4); }
  c.sl[4] = w2slot(4, w);
#pragma unroll
  for (int n = 0; n < 2; ++n) c.aW[n] = (4 * g + q4) * 128 + ((n ^ ((q4 >> 1) & 1)) << 6) + p4 * 16;
  c.vo = (c.rt2 * 64 + 8 * w + (l >> 3)) * 1024 + c.ct2 * 128 + (l & 7) * 16;
  PIN(c.vw2); PIN(c.aW[0]); PIN(c.aW[1]); PIN(c.vo);
  if (w == 0 && l < 4) stl<u32>(T_PAIR + l * 4, 0u);
  const u32 xs = (i >> 1) & 7, tb = R == RQ ? (w & 1) * 2 * LW : ((w >> 1) & 1) * 4 * LW;
  s.aA[0] = tb + i * 128 + ((g ^ xs) << 4); s.aA[1] = s.aA[0] ^ 64;
  s.aH[0] = HALO + i * 128 + ((g ^ xs) << 4); s.aH[1] = s.aH[0] ^ 64;
  const u32 f1 = ((q4 >> 1) & 1) | ((g & 1) << 1), bb = g * LW + 2048 + q4 * 128 + p4 * 8;
  if constexpr (R == RQ) s.aB[0] = bb + (((w >> 1) ^ f1) << 5);
  if constexpr (R == RF0) { for (int n = 0; n < 4; ++n) s.aB[n] = bb + (w & 1) * 1024 + ((n ^ f1) << 5); }
  if constexpr (R == RF1) {
    for (int n = 0; n < 2; ++n) s.aB[n] = bb + 2048 + (((2 * (w & 1) + n) ^ f1) << 5);
    s.aB[2] = HALF_OFF + (g >> 1) * 1024 + (8 * (g & 1) + q4) * 64 + (((w & 1) ^ (g & 1)) << 5) + p4 * 8;
  }
  if constexpr (G == 1) {
    constexpr u32 NC = R == RQ ? 48 : 56;
    const u32 lc = NC * v + (l < NC ? l : 0);
    const u32 gc = R == RQ ? (lc >> 6) * 512 + 64 * c.ci + (lc & 63) : 1536 + (lc < 192 ? colL0(c.ci) + lc : colHf(c.ci) + lc - 192);
    const u32 lr = 36 * v + (l < 36 ? l : 0);
    int row = (int)(c.rt * 128) + (lr < 128 ? (int)tile_row(lr >> 4, lr & 15) : halo_row(lr & 15));
    row = row < 0 ? 0 : row;
    s.vgat[0] = l < NC ? WS_COL + gc * 16 : OOB; s.vgat[1] = l < 36 ? WS_ROW + (u32)row * 16 : OOB;
    s.aTw[0] = l < NC ? T_CS + lc * 4 : T_DUMP + l * 4; s.aTw[1] = T_MEAN + (l < 36 ? lr : 144 + (l & 15)) * (R == RQ ? 4 : 16);
    PIN(s.vgat[0]); PIN(s.vgat[1]); PIN(s.aTw[0]); PIN(s.aTw[1]);
    asm volatile("" : "=v"(s.arr[0]), "=v"(s.arr[1]));
    cols_setup<R>(c, s, p);
  }
  for (int k = 0; k < 2; ++k) { PIN(s.aA[k]); if constexpr (R == RQ) PIN(s.aH[k]); }
  for (int k = 0; k < RC<R>::NBASE; ++k) PIN(s.aB[k]);
  SB();
  asm volatile("buffer_load_dword %0, %1, %2, 0 offen sc1" : "=v"(s.epoch) : "v"(l == 0 ? 0u : OOB), "s"(c.rws) : "memory");
  {
    const u32 wa = c.wg * 8 + w;
    touch(s.wt, l < 6 ? (u64)p.wqkv + (wa * 6 + l) * 128 : (u64)p.w1 + (wa * 7 + (l < 13 ? l - 6 : 0)) * 128, 0x1fff);
  }
  wait_bar<G == 0 ? PRE1 + 2 : 2>();
  s.wa = pro.wa;
  KEEP(pro.ctouch);
  if constexpr (G == 0) { s.rows[0] = landed(pro.rows[0]); s.rows[1] = landed(pro.rows[1]); }
  if constexpr (G == 1) __builtin_amdgcn_s_setprio(1);
  sfor<RC<R>::NA + 2 * RC<R>::NB>([&](auto ic) { frag_read<R, 0, 0, 0, ic>(s); });
  if constexpr (G == 1) sfor<(R == RQ ? 10 : 11) - PRE1>([&](auto ic) { do_op<R, chunk_piece(1, PRE1 + ic)>(c, s); });
  wait_lgkm<0>();
  ticks<R, G>(c, s, mkseq<8>{});
  asm volatile("s_nop 7" ::: "memory");
  if constexpr (R == RQ) {
    PIN(s.tq.csq); PIN(s.tq.csk); PIN(s.tq.csv);
    for (int j = 0; j < 5; ++j) { PIN(s.tq.mi[j]); PIN(s.tq.ri[j]); PIN(s.tq.mr[j]); PIN(s.tq.rr[j]); }
  } else {
    for (int m = 0; m < 4; ++m) PIN(s.tf.tok[m]);
    for (int n = 0; n < RC<R>::NB; ++n) PIN(s.tf.cs[n]);
  }
  epi_head<G>(c);
#pragma unroll
  for (int m = 0; m < RC<R>::NM; ++m) PIN(s.acc[m]);
  if constexpr (R == RQ) epi_qkv(c, s, s.tq); else epi_ffn<R>(c, s, s.tf);
  epi_tail<G>(c);
  if constexpr (G == 0) KEEP(s.touch);
}

struct S2 { u32 fo, fo2, fl, need; f4 acc[16]; v4u ha[4][4]; u32 vh, aP[2][2], aR[2][3]; v4u sel; f4 x0, x1; };
template <int S, int Q> DI void h_load(const Ctx& c, S2& s) { gld_sc1<Q * 1024>(s.ha[S & 3][Q], s.vh, c.rws, hblk(S) * 4096); }
template <int S, int M> DI void gap2(Ctx& c, S2& s, const v4u (&hf)[4]) {
  mfma<S == 0>(s.acc[M], cat(c.wl[S & 3][M >> 2], c.wh[S & 3][M >> 2]), hf[M & 3]);
  if constexpr (S + 4 <= 8 && M >= 4 && (M & 3) < 2) w2_read<S + 4, ((M >> 2) - 1) * 2 + (M & 3)>(c);
  if constexpr (S == 1 && M == 3) { if (c.w == 7) dma_ks<8>(c); }
  if constexpr (S == 2 && M == 0) asm volatile("buffer_load_dword %0, %1, %2, 0 offen sc1" : "=v"(s.fl) : "v"(s.fo2), "s"(c.rws) : "memory");
  if constexpr (S >= 2 && S <= 6 && (M & 3) == 2) h_load<S + 2, (M >> 2)>(c, s);
  if constexpr (S < 2 && M >= 8 && (M & 1) == 0) h_load<S + 2, ((M - 8) >> 1)>(c, s);
}
DI void poll(const Ctx& c, S2& s, u32 fo, u32 need) {
#pragma unroll 1
  for (u32 spin = 0; spin < SPIN_CAP; ++spin) {
    asm volatile("buffer_load_dword %0, %1, %2, 0 offen sc1\n\ts_waitcnt vmcnt(0)" : "=&v"(s.fl) : "v"(fo), "s"(c.rws) : "memory");
    if (((u32)__builtin_amdgcn_ballot_w64(s.fl == c.tag) & need) == need) break;
    __builtin_amdgcn_s_sleep(3);
  }
}
template <int S, int... Ms> DI void step2(Ctx& c, S2& s, iseq<Ms...>) {
  constexpr int NV = S < 8 ? 4 : 0;
  v4u(&a)[4] = s.ha[S & 3];
  if constexpr (S >= 4 || S == 1) asm volatile("s_waitcnt vmcnt(%5) lgkmcnt(%6)" : "+v"(a[0]), "+v"(a[1]), "+v"(a[2]), "+v"(a[3]), "+v"(s.fl) : "i"(NV), "i"(S == 4 ? 8 : 0) : "memory");
  else asm volatile("s_waitcnt vmcnt(%5)" : "+v"(a[0]), "+v"(a[1]), "+v"(a[2]), "+v"(a[3]), "+v"(s.fl) : "i"(NV) : "memory");
  if constexpr (S == 3) { if (((u32)__builtin_amdgcn_ballot_w64(s.fl == c.tag) & (s.need << 4)) != (s.need << 4)) poll(c, s, s.fo2, s.need << 4); }
  v4u hf[4];
  if constexpr (hblk(S) < 6) { hf[0] = a[0]; hf[1] = a[1]; hf[2] = a[2]; hf[3] = a[3]; }
  else {
    hf[0] = (v4u){a[0].x, a[0].y, a[2].x, a[2].y}; hf[1] = (v4u){a[0].z, a[0].w, a[2].z, a[2].w};
    hf[2] = (v4u){a[1].x, a[1].y, a[3].x, a[3].y}; hf[3] = (v4u){a[1].z, a[1].w, a[3].z, a[3].w};
    asm volatile("s_nop 1" : "+v"(hf[0]), "+v"(hf[1]), "+v"(hf[2]), "+v"(hf[3]));
  }
  (gap2<S, Ms>(c, s, hf), ...);
  if constexpr (S + 4 <= 8) { w2_read<S + 4, 6>(c); w2_read<S + 4, 7>(c); }
}

DI void stage2(Ctx& c, const Params& p) {
  S2 s;
  asm volatile("" : "+s"(c.w));
  const u32 l = c.lane, w = c.w, g = c.g, i = c.i16;
#pragma unroll
  for (int k = 0; k < 5; ++k) c.sl[k] = w2slot(k, w);
  s.vh = WS_H + c.rt2 * H_RT + w * H_CI + l * 16;
  s.need = (3u << (2 * (c.rt2 & 1))) | 0xf00u;
  {
    const u32 fo = WS_FLAG + (((c.rt2 >> 1) * 16 + w * 2) * 8 + i) * 128, bit = l < 16 ? 1u << l : 0u;
    s.fo = (s.need & bit) ? fo : OOB; s.fo2 = ((s.need << 4) & bit) ? fo : OOB;
  }
  {
    PIN(c.xr);
    s.x0 = (f4){bflo(c.xr.x), bfhi(c.xr.x), bflo(c.xr.y), bfhi(c.xr.y)}; s.x1 = (f4){bflo(c.xr.z), bfhi(c.xr.z), bflo(c.xr.w), bfhi(c.xr.w)};
#pragma unroll
    for (int h = 0; h < 2; ++h)
#pragma unroll
      for (int P = 0; P < 2; ++P) { s.aP[h][P] = c.sl[h] + (i ^ h) * 128 + (((4 * P + g) ^ (i & 7)) << 4); PIN(s.aP[h][P]); }
#pragma unroll
    for (int q = 0; q < 2; ++q) {
      const u32 it = 32 * (g >> 1) + 16 * q + i, row = 8 * w + (it >> 3), ri = (row >> 1) & 15, rm = ((row & 1) << 1) | (row >> 5);
      const u32 b = (rm < 2 ? w2slot(0, 0) : w2slot(1, 0)) + (rm & 1) * 2048 + (ri ^ (rm >> 1)) * 128 + (((it & 7) ^ (ri & 7)) << 4) + (g & 1) * 4096;
      s.aR[q][0] = b; s.aR[q][1] = b + ((rm >= 2 && (g & 1)) ? 4096 : 0); s.aR[q][2] = b + (rm >= 2 ? 4096 : 0);
      PIN(s.aR[q][0]); PIN(s.aR[q][1]); PIN(s.aR[q][2]);
    }
#pragma unroll
    for (int e = 0; e < 4; ++e) s.sel[e] = (((i & 7) >> 1) == (u32)e && (g >> 1) == (i >> 3)) ? ((i & 1) ? 0x3f800000u : 0x00003f80u) : 0u;
    PIN(s.sel); PIN(s.x0); PIN(s.x1);
  }
  if (w >= 4) { wait_vm<0>(); publish(c); dma_ks<6>(c); dma_ks<7>(c); if (w < 7) dma_ks<8>(c); }
  poll(c, s, s.fo, s.need);
  if (w >= 4) __builtin_amdgcn_s_setprio(1);
  sfor<8>([&](auto ic) { h_load<(ic >> 2), (ic & 3)>(c, s); });
  sfor<9>([&](auto ic) { step2<ic>(c, s, mkseq<16>{}); });
  asm volatile("s_nop 7\n\ts_nop 7" ::: "memory");
#pragma unroll
  for (int m = 0; m < 16; ++m) PIN(s.acc[m]);
#pragma unroll
  for (int m = 0; m < 4; ++m)
#pragma unroll
    for (int P = 0; P < 2; ++P) {
      const f4 a = s.acc[2 * P * 4 + m], b = s.acc[(2 * P + 1) * 4 + m];
      v4u d = {cvtpk(a[0], a[1]), cvtpk(a[2], a[3]), cvtpk(b[0], b[1]), cvtpk(b[2], b[3])};
      PIN(d);
      if (m & 1) asm volatile("ds_write_b128 %0, %1 offset:2048" ::"v"(s.aP[m >> 1][P]), "v"(d) : "memory");
      else asm volatile("ds_write_b128 %0, %1" ::"v"(s.aP[m >> 1][P]), "v"(d) : "memory");
    }
  bar();
  v4u pr[8];
  sfor<8>([&](auto kc) { constexpr int k = kc, t = k & 3; rd128<t * 8192>(pr[k], s.aR[k >> 2][t < 2 ? t : 2]); });
  asm volatile("s_waitcnt lgkmcnt(0)" : "+v"(pr[0]), "+v"(pr[1]), "+v"(pr[2]), "+v"(pr[3]), "+v"(pr[4]), "+v"(pr[5]), "+v"(pr[6]), "+v"(pr[7])::"memory");
  f4 d0 = {0.f, 0.f, 0.f, 0.f}, d1 = d0;
#pragma unroll
  for (int t = 0; t < 4; ++t) { d0 = mfma_b(s.sel, pr[t], d0); d1 = mfma_b(s.sel, pr[4 + t], d1); }
  f4 a0, a1;
#pragma unroll
  for (int r = 0; r < 4; ++r) {
    auto t = __builtin_amdgcn_permlane16_swap(__float_as_uint(d0[r]), __float_as_uint(d1[r]), false, false);
    a0[r] = __uint_as_float(t[0]); a1[r] = __uint_as_float(t[1]);
  }
  a0 += s.x0; a1 += s.x1;
  gst_sc1<0>((v4u){cvtpk(a0[0], a0[1]), cvtpk(a0[2], a0[3]), cvtpk(a1[0], a1[1]), cvtpk(a1[2], a1[3])}, c.vo, mkrsrc(p.out, 2048 * 1024), 0);
  if (c.wg == 0 && w == 7 && !c.retired) {
#pragma unroll 1
    for (u32 spin = 0; spin < SPIN_CAP; ++spin) {
      v4u a[2];
      asm volatile("buffer_load_dwordx4 %0, %2, %3, 0 offen sc1\n\tbuffer_load_dwordx4 %1, %2, %3, 0 offen offset:16 sc1\n\ts_waitcnt vmcnt(0)"
                   : "=&v"(a[0]), "=&v"(a[1]) : "v"(WS_ARR + l * 32), "s"(c.rws) : "memory");
      if (retire(c, a)) break;
      __builtin_amdgcn_s_sleep(1);
    }
  }
}

__global__ void __launch_bounds__(512, 2) fused_block(Params p) {
  Ctx c;
  Pro pro;
  c.lane = threadIdx.x & 63;
  {
    u64 pc;
    u32 len;
    asm volatile("s_getpc_b64 %0\nfused_block_code_begin:\n\ts_mov_b32 %1, fused_block_code_end-fused_block_code_begin" : "=s"(pc), "=s"(len));
    const u32 o = ((blockIdx.x >> 3) * 16 + (threadIdx.x >> 6) * 2 + (threadIdx.x & 1)) * 128;
    asm volatile("s_mov_b64 exec, 3\n\tglobal_load_dword %0, %1, %2\n\ts_mov_b64 exec, -1" : "=v"(pro.ctouch) : "v"(o < len ? o : 0u), "s"(pc) : "memory");
  }
  c.g = c.lane >> 4; c.i16 = c.lane & 15;
  c.retired = false;
  c.w = rfl(threadIdx.x >> 6); c.wg = blockIdx.x;
  const u32 xcd = c.wg & 7, j = c.wg >> 3;
  c.rt = (xcd >> 2) * 8 + (j >> 2); c.ci = (xcd & 3) * 2 + ((j >> 1) & 1);
  c.rt2 = xcd * 4 + (j >> 3); c.ct2 = j & 7;
  c.krot = (xcd + ((j >> 2) & 3) * 2) & 7;
  asm volatile("" ::"s"(p.x), "s"(p.wqkv), "s"(p.wo), "s"(p.w1), "s"(p.w2), "s"(p.out), "s"(p.ws));
  c.rx = mkrsrc(p.x, 2048 * 1024);
  prologue(c, p, pro, j & 1);
  SB();
  c.rws = mkrsrc(p.ws, WS_USED); c.rw2 = mkrsrc(p.w2, 1792 * 1024); c.rwo = mkrsrc(p.wo, 512 * 1024);
  if (j & 1) { if (c.w < 4) stage1<RQ, 0>(c, p, pro); else stage1<RQ, 1>(c, p, pro); }
  else if (c.w < 4) stage1<RF0, 0>(c, p, pro);
  else stage1<RF1, 1>(c, p, pro);
  stage2(c, p);
  asm volatile("fused_block_code_end:");
}

extern "C" void kernel_launch(void* const* d_in, const int* in_sizes, int n_in, void* d_out, int out_size, void* d_ws, size_t ws_size, hipStream_t stream) {
  Params p;
  p.x = (const char*)d_in[0]; p.wqkv = (const char*)d_in[1]; p.wo = (const char*)d_in[2];
  p.w1 = (const char*)d_in[4]; p.w2 = (const char*)d_in[5];
  p.out = (char*)d_out; p.ws = (char*)d_ws;
  (void)hipFuncSetAttribute((const void*)fused_block, hipFuncAttributeMaxDynamicSharedMemorySize, LDS_BYTES);
  fused_block<<<256, 512, LDS_BYTES, stream>>>(p);
}
```
